```python
import torch

def _softmax(x, axis=-1):
    return torch.softmax(x, dim=axis)
def _gelu(x):
    return torch.nn.functional.gelu(x, approximate="tanh")
def _silu(x):
    return torch.nn.functional.silu(x)
def _sigmoid(x):
    return torch.sigmoid(x)
def _layernorm(x, axis=-1, eps=1e-5):
    m = x.mean(dim=axis, keepdim=True)
    v = x.var(dim=axis, keepdim=True, unbiased=False)
    return (x - m) / torch.sqrt(v + eps)
def _scatter_add(tgt, idx, src, axis=0):
    return tgt.clone().index_add_(axis, idx, src)
def _segment_sum(src, seg, n):
    out = torch.zeros((n,) + src.shape[1:], dtype=src.dtype)
    return out.index_add_(0, seg, src)
def _pad(x, pw):
    flat = tuple(v for pair in reversed(pw) for v in pair)
    return torch.nn.functional.pad(x, flat)

kernel_name = 'concat_heads__mha_block_cached__compressed_kv_cla__decode'

def setup_inputs():

    _w = (lambda a: (lambda t: t.to(torch.bfloat16) if t.is_floating_point() else t)(torch.as_tensor(a)))
    def _A_setup():
        B, Sq, D = 2, 1, 512
        H, Sk, Dh = 32, 4096, 128
        return {
            "x": _w((lambda *s: torch.randn(*s, dtype=torch.bfloat16) * 1.6)(B, Sq, D)),
            "k_cache": _w((lambda *s: torch.randn(*s, dtype=torch.bfloat16) * 1.6)(B, H, Sk, Dh)),
            "v_cache": _w((lambda *s: torch.randn(*s, dtype=torch.bfloat16) * 0.05)(B, H, Sk, Dh)),
            "wq": _w((lambda *s: torch.randn(*s, dtype=torch.bfloat16) * 1.6)(D, H * Dh) * (D ** -0.5)),
            "wo": _w((lambda *s: torch.randn(*s, dtype=torch.bfloat16) * 0.05)(H * Dh, D) * ((H * Dh) ** -0.5)),
        }
    _A_in = _A_setup()

    def _B_setup():
        B, H, Sq, Sk, D, M = 2, 32, 1, 4096, 128, 512
        Dc = 4 * D
        return {
            "x": _w((lambda *s: torch.randn(*s, dtype=torch.bfloat16) * 3.2)(B, Sq, M)),
            "c_kv": _w((lambda *s: torch.randn(*s, dtype=torch.bfloat16) * 0.1)(B, Sk, Dc)),
            "w_q": _w((lambda *s: torch.randn(*s, dtype=torch.bfloat16) * 6.4)(M, H, D) * (M ** -0.5)),
            "w_uk": _w((lambda *s: torch.randn(*s, dtype=torch.bfloat16) * 6.4)(Dc, H, D) * (Dc ** -0.5)),
            "w_uv": _w((lambda *s: torch.randn(*s, dtype=torch.bfloat16) * 0.1)(Dc, H, D) * (Dc ** -0.5)),
            "w_o": _w((lambda *s: torch.randn(*s, dtype=torch.bfloat16) * 0.1)(H, D, M) * ((H * D) ** -0.5)),
        }
    _B_in = _B_setup()

    return {
        "A_x": _A_in["x"],
        "A_k_cache": _A_in["k_cache"],
        "A_v_cache": _A_in["v_cache"],
        "A_wq": _A_in["wq"],
        "A_wo": _A_in["wo"],
        "B_x": _B_in["x"],
        "B_c_kv": _B_in["c_kv"],
        "B_w_q": _B_in["w_q"],
        "B_w_uk": _B_in["w_uk"],
        "B_w_uv": _B_in["w_uv"],
        "B_w_o": _B_in["w_o"],
    }

def reference(A_x, A_k_cache, A_v_cache, A_wq, A_wo, B_x, B_c_kv, B_w_q, B_w_uk, B_w_uv, B_w_o):
    def _A_ref(x, k_cache, v_cache, wq, wo):
        B, Sq, D = x.shape
        H, Dh = 32, 128
        q = (lambda x, *d: x.permute(*d))((x @ wq).reshape(B, Sq, H, Dh), 0, 2, 1, 3)
        scores = torch.einsum("bhqd,bhkd->bhqk", q, k_cache) * (Dh ** -0.5)
        p = _softmax(scores, axis=-1)
        o = torch.einsum("bhqk,bhkd->bhqd", p, v_cache)
        return (lambda x, *d: x.permute(*d))(o, 0, 2, 1, 3).reshape(B, Sq, H * Dh) @ wo

    def _B_ref(x, c_kv, w_q, w_uk, w_uv, w_o):
        B, Sq, M = x.shape[0], x.shape[1], x.shape[2]
        Sk = c_kv.shape[1]
        H, D = w_q.shape[1], w_q.shape[2]
        q = torch.einsum("bqm,mhd->bhqd", x, w_q)
        k = torch.einsum("bkc,chd->bhkd", c_kv, w_uk)
        v = torch.einsum("bkc,chd->bhkd", c_kv, w_uv)
        scores = torch.einsum("bhqd,bhkd->bhqk", q, k) * (D ** -0.5)
        off = Sk - Sq
        qi = torch.arange(Sq)[:, None]
        kj = torch.arange(Sk)[None, :]
        cmask = kj <= (qi + off)
        scores = torch.where(cmask[None, None], scores, -1e9)
        p = _softmax(scores, axis=-1)
        ctx = torch.einsum("bhqk,bhkd->bhqd", p, v)
        return torch.einsum("bhqd,hdm->bqm", ctx, w_o)

    a = _A_ref(A_x, A_k_cache, A_v_cache, A_wq, A_wo)
    b = _B_ref(B_x, B_c_kv, B_w_q, B_w_uk, B_w_uv, B_w_o)
    return torch.concatenate([a, b], axis=-1)
```

```cpp
#include <hip/hip_runtime.h>
#include <hip/hip_bf16.h>
#include <stdint.h>

typedef unsigned short u16;

__device__ __forceinline__ float bf2f(u16 v) { return __uint_as_float(((unsigned)v) << 16); }
__device__ __forceinline__ u16 f2bf(float f) {
    unsigned u = __float_as_uint(f);
    if ((u & 0x7fffffffu) > 0x7f800000u) return (u16)((u >> 16) | 0x40);
    u += 0x7fffu + ((u >> 16) & 1u);
    return (u16)(u >> 16);
}

constexpr int BATCH = 2, H = 32, SK = 4096, DH = 128, DM = 512, DC = 512;
constexpr float SCALE = 0.08838834764831845f;

__global__ void k_q(const u16* x, const u16* W, float* q) {
    int n = blockIdx.x * blockDim.x + threadIdx.x;
    int b = blockIdx.y;
    float acc = 0.f;
    for (int m = 0; m < DM; ++m) acc += bf2f(x[b * DM + m]) * bf2f(W[(size_t)m * (H * DH) + n]);
    q[b * (H * DH) + n] = bf2f(f2bf(acc));
}

__global__ void k_qabs(const u16* wuk, const float* q, float* qabs) {
    int c = blockIdx.x * blockDim.x + threadIdx.x;
    int h = blockIdx.y, b = blockIdx.z;
    float acc = 0.f;
    for (int d = 0; d < DH; ++d) acc += bf2f(wuk[((size_t)c * H + h) * DH + d]) * q[(b * H + h) * DH + d];
    qabs[(b * H + h) * DC + c] = acc;
}

__global__ void k_attn(const float* qv, int qstride_bh, const u16* kbase, size_t kstride_b, size_t kstride_h, int KD,
                       const u16* vbase, size_t vstride_b, size_t vstride_h, int VD, float* out) {
    __shared__ float sc[SK];
    __shared__ float red[256];
    int h = blockIdx.x, b = blockIdx.y, t = threadIdx.x;
    const float* q = qv + (size_t)(b * H + h) * qstride_bh;
    const u16* K = kbase + b * kstride_b + h * kstride_h;
    const u16* V = vbase + b * vstride_b + h * vstride_h;
    float lmax = -INFINITY;
    for (int k = t; k < SK; k += 256) {
        float acc = 0.f;
        for (int d = 0; d < KD; ++d) acc += q[d] * bf2f(K[(size_t)k * KD + d]);
        acc *= SCALE;
        sc[k] = acc;
        lmax = fmaxf(lmax, acc);
    }
    red[t] = lmax; __syncthreads();
    for (int s = 128; s > 0; s >>= 1) { if (t < s) red[t] = fmaxf(red[t], red[t + s]); __syncthreads(); }
    float M = red[0]; __syncthreads();
    float lsum = 0.f;
    for (int k = t; k < SK; k += 256) { float p = __expf(sc[k] - M); sc[k] = p; lsum += p; }
    red[t] = lsum; __syncthreads();
    for (int s = 128; s > 0; s >>= 1) { if (t < s) red[t] += red[t + s]; __syncthreads(); }
    float L = red[0];
    __syncthreads();
    for (int d = t; d < VD; d += 256) {
        float acc = 0.f;
        for (int k = 0; k < SK; ++k) acc += sc[k] * bf2f(V[(size_t)k * VD + d]);
        out[(size_t)(b * H + h) * VD + d] = acc / L;
    }
}

__global__ void k_uv(const float* ctxc, const u16* wuv, float* ctx) {
    int d = threadIdx.x, h = blockIdx.x, b = blockIdx.y;
    float acc = 0.f;
    for (int c = 0; c < DC; ++c) acc += ctxc[(b * H + h) * DC + c] * bf2f(wuv[((size_t)c * H + h) * DH + d]);
    ctx[(b * H + h) * DH + d] = acc;
}

__global__ void k_oproj(const float* o, const u16* Wo, u16* out, int off) {
    int n = blockIdx.x * blockDim.x + threadIdx.x, b = blockIdx.y;
    float acc = 0.f;
    for (int k = 0; k < H * DH; ++k) acc += o[b * H * DH + k] * bf2f(Wo[(size_t)k * DM + n]);
    out[b * 1024 + off + n] = f2bf(acc);
}

extern "C" void kernel_launch(void* const* d_in, const int* in_sizes, int n_in,
                              void* d_out, int out_size, void* d_ws, size_t ws_size,
                              hipStream_t stream) {
    const u16* A_x = (const u16*)d_in[0];
    const u16* A_k = (const u16*)d_in[1];
    const u16* A_v = (const u16*)d_in[2];
    const u16* A_wq = (const u16*)d_in[3];
    const u16* A_wo = (const u16*)d_in[4];
    const u16* B_x = (const u16*)d_in[5];
    const u16* B_ckv = (const u16*)d_in[6];
    const u16* B_wq = (const u16*)d_in[7];
    const u16* B_wuk = (const u16*)d_in[8];
    const u16* B_wuv = (const u16*)d_in[9];
    const u16* B_wo = (const u16*)d_in[10];
    u16* out = (u16*)d_out;
    float* ws = (float*)d_ws;
    float* qA = ws;
    float* qB = qA + 8192;
    float* qabs = qB + 8192;
    float* oA = qabs + 32768;
    float* ctxc = oA + 8192;
    float* ctx = ctxc + 32768;

    k_q<<<dim3(16, 2), 256, 0, stream>>>(A_x, A_wq, qA);
    k_q<<<dim3(16, 2), 256, 0, stream>>>(B_x, B_wq, qB);
    k_qabs<<<dim3(2, 32, 2), 256, 0, stream>>>(B_wuk, qB, qabs);
    k_attn<<<dim3(32, 2), 256, 0, stream>>>(qA, DH, A_k, (size_t)H * SK * DH, (size_t)SK * DH, DH,
                                            A_v, (size_t)H * SK * DH, (size_t)SK * DH, DH, oA);
    k_attn<<<dim3(32, 2), 256, 0, stream>>>(qabs, DC, B_ckv, (size_t)SK * DC, 0, DC,
                                            B_ckv, (size_t)SK * DC, 0, DC, ctxc);
    k_uv<<<dim3(32, 2), 128, 0, stream>>>(ctxc, B_wuv, ctx);
    k_oproj<<<dim3(2, 2), 256, 0, stream>>>(oA, A_wo, out, 0);
    k_oproj<<<dim3(2, 2), 256, 0, stream>>>(ctx, B_wo, out, 512);
}
```

```cpp
#include <hip/hip_runtime.h>
#include <stdint.h>

typedef unsigned short u16;
typedef unsigned int u32;
using u32x4 = __attribute__((ext_vector_type(4))) unsigned;
using u32x2 = __attribute__((ext_vector_type(2))) unsigned;
using f32x4 = __attribute__((ext_vector_type(4))) float;
using bf16x2 = __attribute__((ext_vector_type(2))) __bf16;
using bf16x8 = __attribute__((ext_vector_type(8))) __bf16;
using s16x4 = __attribute__((ext_vector_type(4))) short;
typedef __attribute__((address_space(3))) s16x4 lds_s16x4;

constexpr float SCALE = 0.08838834764831845f;
constexpr float C2 = SCALE * 1.4426950408889634f;

__device__ __forceinline__ float bflo(u32 v) { return __uint_as_float(v << 16); }
__device__ __forceinline__ float bfhi(u32 v) { return __uint_as_float(v & 0xffff0000u); }
__device__ __forceinline__ float bf2f(u16 v) { return __uint_as_float(((u32)v) << 16); }
__device__ __forceinline__ u16 f2bf(float f) {
    u32 u = __float_as_uint(f);
    u += 0x7fffu + ((u >> 16) & 1u);
    return (u16)(u >> 16);
}
__device__ __forceinline__ float dot2(u32 a, u32 b, float acc) {
    return __builtin_amdgcn_fdot2_f32_bf16(__builtin_bit_cast(bf16x2, a), __builtin_bit_cast(bf16x2, b), acc, false);
}
template <int CTRL> __device__ __forceinline__ float dpp(float x) {
    return __builtin_bit_cast(float, __builtin_amdgcn_mov_dpp(__builtin_bit_cast(int, x), CTRL, 0xf, 0xf, true));
}
constexpr int XOR1 = 0xB1, XOR2 = 0x4E, XOR7 = 0x141, XOR8 = 0x128, ROR4 = 0x124;
__device__ __forceinline__ u32x4 ldg16(const u16* p) { return *reinterpret_cast<const u32x4*>(p); }
__device__ __forceinline__ u32x4 ldg16nt(const u16* p) { return __builtin_nontemporal_load(reinterpret_cast<const u32x4*>(p)); }
__device__ __forceinline__ float exp2a(float x) { return __builtin_amdgcn_exp2f(x); }
#define SCHED_FENCE() __builtin_amdgcn_sched_barrier(0)

struct Params {
    const u16 *A_x, *A_k, *A_v, *A_wq, *A_wo;
    const u16 *B_x, *B_ckv, *B_wq, *B_wuk, *B_wuv, *B_wo;
    u32* flags;
    u16* qabs;
    float* partB;
    float* statB;
    float* yB;
    float* yA;
    float* statA;
};


__device__ __forceinline__ void f1_partial(const u32x4 (&f)[16], const float2* xs, float* red, int wave, int lane) {
    const int r = lane >> 4, c = lane & 15;
    float a0[8], a1[8];
#pragma unroll
    for (int k = 0; k < 8; ++k) { a0[k] = 0.f; a1[k] = 0.f; }
#pragma unroll
    for (int i = 0; i < 16; ++i) {
        const float2 xv = xs[64 * wave + 4 * i + r];
#pragma unroll
        for (int k = 0; k < 4; ++k) {
            const float lo = bflo(f[i][k]), hi = bfhi(f[i][k]);
            a0[2 * k] = fmaf(xv.x, lo, a0[2 * k]); a0[2 * k + 1] = fmaf(xv.x, hi, a0[2 * k + 1]);
            a1[2 * k] = fmaf(xv.y, lo, a1[2 * k]); a1[2 * k + 1] = fmaf(xv.y, hi, a1[2 * k + 1]);
        }
    }
#pragma unroll
    for (int k = 0; k < 8; ++k) {
        a0[k] += __shfl_xor(a0[k], 16); a0[k] += __shfl_xor(a0[k], 32);
        a1[k] += __shfl_xor(a1[k], 16); a1[k] += __shfl_xor(a1[k], 32);
    }
    if (r == 0) {
        float* d0 = red + (wave * 2 + 0) * 128 + 8 * c;
        float* d1 = red + (wave * 2 + 1) * 128 + 8 * c;
        *reinterpret_cast<f32x4*>(d0) = f32x4{a0[0], a0[1], a0[2], a0[3]};
        *reinterpret_cast<f32x4*>(d0 + 4) = f32x4{a0[4], a0[5], a0[6], a0[7]};
        *reinterpret_cast<f32x4*>(d1) = f32x4{a1[0], a1[1], a1[2], a1[3]};
        *reinterpret_cast<f32x4*>(d1 + 4) = f32x4{a1[4], a1[5], a1[6], a1[7]};
    }
}
__device__ __forceinline__ float f1_sum(const float* red, int b, int d) {
    float s = 0.f;
#pragma unroll
    for (int w = 0; w < 8; ++w) s += red[(w * 2 + b) * 128 + d];
    return s;
}

__device__ __forceinline__ void f2_partial(const u32x4 (&f)[16], const float2* os, float* ybuf, int wave, int lane) {
    float a0[8], a1[8];
#pragma unroll
    for (int k = 0; k < 8; ++k) { a0[k] = 0.f; a1[k] = 0.f; }
#pragma unroll
    for (int i = 0; i < 16; ++i) {
        const float2 ov = os[16 * wave + i];
#pragma unroll
        for (int k = 0; k < 4; ++k) {
            const float lo = bflo(f[i][k]), hi = bfhi(f[i][k]);
            a0[2 * k] = fmaf(ov.x, lo, a0[2 * k]); a0[2 * k + 1] = fmaf(ov.x, hi, a0[2 * k + 1]);
            a1[2 * k] = fmaf(ov.y, lo, a1[2 * k]); a1[2 * k + 1] = fmaf(ov.y, hi, a1[2 * k + 1]);
        }
    }
    float* d0 = ybuf + (wave * 2 + 0) * 512 + 8 * lane;
    float* d1 = ybuf + (wave * 2 + 1) * 512 + 8 * lane;
    *reinterpret_cast<f32x4*>(d0) = f32x4{a0[0], a0[1], a0[2], a0[3]};
    *reinterpret_cast<f32x4*>(d0 + 4) = f32x4{a0[4], a0[5], a0[6], a0[7]};
    *reinterpret_cast<f32x4*>(d1) = f32x4{a1[0], a1[1], a1[2], a1[3]};
    *reinterpret_cast<f32x4*>(d1 + 4) = f32x4{a1[4], a1[5], a1[6], a1[7]};
}
__device__ __forceinline__ float f2_sum(const float* ybuf, int seg, int n) {
    float s = 0.f;
#pragma unroll
    for (int w = 0; w < 8; ++w) s += ybuf[(w * 2 + seg) * 512 + n];
    return s;
}

struct Page { u32x4 k[4], v[4]; };

constexpr int A_LDS_XS = 0;
constexpr int A_LDS_RED = 4096;
constexpr int A_LDS_QB = A_LDS_RED + 8192;
constexpr int A_LDS_WREC = A_LDS_QB + 512;
constexpr int A_LDS_OS = A_LDS_WREC + 4352;
constexpr int A_LDS_YBUF = A_LDS_OS + 1024;
constexpr int A_LDS_END = A_LDS_YBUF + 32768;

__device__ __forceinline__ void roleA(const Params& p, const int h, const int j, unsigned char* smem) {
    const int tid = threadIdx.x, lane = tid & 63, wave = __builtin_amdgcn_readfirstlane(tid >> 6);
    const int r = lane >> 4, c = lane & 15;
    const int g = 8 * j + wave;
    const int b = g >= 28 ? 1 : 0;
    const int gi = g - 28 * b;
    const int q0 = (256 * gi) / 7, q1 = (256 * (gi + 1)) / 7;
    const bool has_tail = (q1 - q0) == 37;
    const size_t kvoff = ((size_t)(b * 32 + h) * 4096 + 4 * q0) * 128 + r * 128 + 8 * c;
    const u16* Kp = p.A_k + kvoff;
    const u16* Vp = p.A_v + kvoff;

    float2* xs = reinterpret_cast<float2*>(smem + A_LDS_XS);
    float* red = reinterpret_cast<float*>(smem + A_LDS_RED);
    u16* qb = reinterpret_cast<u16*>(smem + A_LDS_QB);
    float* wrec = reinterpret_cast<float*>(smem + A_LDS_WREC);
    float2* os = reinterpret_cast<float2*>(smem + A_LDS_OS);
    float* ybuf = reinterpret_cast<float*>(smem + A_LDS_YBUF);

    const u16 x0 = p.A_x[tid], x1 = p.A_x[512 + tid];
    SCHED_FENCE();
    u32x4 wf[16];
    {
        const u16* W = p.A_wq + (size_t)(64 * wave + r) * 4096 + h * 128 + 8 * c;
#pragma unroll
        for (int i = 0; i < 16; ++i) wf[i] = ldg16(W + (size_t)(4 * i) * 4096);
    }
    Page pg[3];
#define A_ISSUE(S, PAGE) do { _Pragma("unroll") for (int t_ = 0; t_ < 4; ++t_) { \
        pg[S].k[t_] = ldg16nt(Kp + (size_t)((PAGE) * 16 + 4 * t_) * 128); \
        pg[S].v[t_] = ldg16nt(Vp + (size_t)((PAGE) * 16 + 4 * t_) * 128); } } while (0)
    A_ISSUE(0, 0); A_ISSUE(1, 1); A_ISSUE(2, 2);
    SCHED_FENCE();

    xs[tid] = make_float2(bf2f(x0), bf2f(x1));
    __syncthreads();
    f1_partial(wf, xs, red, wave, lane);
    __syncthreads();
    if (tid < 256) qb[tid] = f2bf(f1_sum(red, tid >> 7, tid & 127));
    __syncthreads();
    const u32x4 qv = *reinterpret_cast<const u32x4*>(qb + b * 128 + 8 * c);
    SCHED_FENCE();

    u32x4 of[16];
    const u16* Wo = p.A_wo + (size_t)(h * 128 + 16 * wave) * 512 + 8 * lane;

    float m = -INFINITY, l = 0.f, acc[8];
#pragma unroll
    for (int k = 0; k < 8; ++k) acc[k] = 0.f;

#pragma unroll
    for (int i = 0; i < 9; ++i) {
        const Page& P = pg[i % 3];
        float x[4];
#pragma unroll
        for (int t = 0; t < 4; ++t) {
            float d = dot2(P.k[t][0], qv[0], 0.f);
            d = dot2(P.k[t][1], qv[1], d); d = dot2(P.k[t][2], qv[2], d); d = dot2(P.k[t][3], qv[3], d);
            x[t] = d;
        }
        const float t1 = x[0] + dpp<XOR8>(x[0]), t2 = x[2] + dpp<XOR8>(x[2]);
        const float y0 = (c & 8) ? t2 : t1;
        const float t3 = x[1] + dpp<XOR8>(x[1]), t4 = x[3] + dpp<XOR8>(x[3]);
        const float y1 = (c & 8) ? t4 : t3;
        const float t5 = y0 + dpp<XOR7>(y0), t6 = y1 + dpp<XOR7>(y1);
        float z = (c & 4) ? t6 : t5;
        z += dpp<XOR2>(z); z += dpp<XOR1>(z);
        const float s = z * C2;
        float pm = fmaxf(s, dpp<ROR4>(s)); pm = fmaxf(pm, dpp<XOR8>(pm));
        const float mn = fmaxf(m, pm);
        const float alpha = exp2a(m - mn), pp = exp2a(s - mn);
        l = fmaf(l, alpha, pp); m = mn;
        const float p0 = dpp<0x150>(pp), p1 = dpp<0x154>(pp), p2 = dpp<0x158>(pp), p3 = dpp<0x15C>(pp);
#pragma unroll
        for (int k = 0; k < 4; ++k) {
            float a0 = acc[2 * k] * alpha, a1 = acc[2 * k + 1] * alpha;
            a0 = fmaf(p0, bflo(P.v[0][k]), a0); a1 = fmaf(p0, bfhi(P.v[0][k]), a1);
            a0 = fmaf(p1, bflo(P.v[1][k]), a0); a1 = fmaf(p1, bfhi(P.v[1][k]), a1);
            a0 = fmaf(p2, bflo(P.v[2][k]), a0); a1 = fmaf(p2, bfhi(P.v[2][k]), a1);
            a0 = fmaf(p3, bflo(P.v[3][k]), a0); a1 = fmaf(p3, bfhi(P.v[3][k]), a1);
            acc[2 * k] = a0; acc[2 * k + 1] = a1;
        }
        asm volatile("" : "+v"(acc[0]), "+v"(acc[1]), "+v"(acc[2]), "+v"(acc[3]), "+v"(acc[4]), "+v"(acc[5]), "+v"(acc[6]), "+v"(acc[7]));
        SCHED_FENCE();
        if (i + 3 < 9) A_ISSUE(i % 3, i + 3);
        else if (i == 6 && has_tail) { pg[0].k[0] = ldg16nt(Kp + (size_t)(9 * 16) * 128); pg[0].v[0] = ldg16nt(Vp + (size_t)(9 * 16) * 128); }
        if (i == 6 || i == 7) {
#pragma unroll
            for (int ii = 0; ii < 8; ++ii) of[8 * (i - 6) + ii] = ldg16(Wo + (size_t)(8 * (i - 6) + ii) * 512);
        }
        SCHED_FENCE();
    }
#undef A_ISSUE
    if (has_tail) {
        const Page& P = pg[0];
        float d = dot2(P.k[0][0], qv[0], 0.f);
        d = dot2(P.k[0][1], qv[1], d); d = dot2(P.k[0][2], qv[2], d); d = dot2(P.k[0][3], qv[3], d);
        d += dpp<XOR8>(d); d += dpp<XOR7>(d); d += dpp<XOR2>(d); d += dpp<XOR1>(d);
        const float s = d * C2;
        const float mn = fmaxf(m, s);
        const float alpha = exp2a(m - mn), pp = exp2a(s - mn);
        l = fmaf(l, alpha, (c < 4) ? pp : 0.f); m = mn;
#pragma unroll
        for (int k = 0; k < 4; ++k) {
            acc[2 * k] = fmaf(pp, bflo(P.v[0][k]), acc[2 * k] * alpha);
            acc[2 * k + 1] = fmaf(pp, bfhi(P.v[0][k]), acc[2 * k + 1] * alpha);
        }
    }

    float Mw = fmaxf(m, __shfl_xor(m, 16)); Mw = fmaxf(Mw, __shfl_xor(Mw, 32));
    const float fsc = exp2a(m - Mw);
#pragma unroll
    for (int k = 0; k < 8; ++k) {
        float a = acc[k] * fsc;
        a += __shfl_xor(a, 16); a += __shfl_xor(a, 32);
        acc[k] = a;
    }
    float lw = l * fsc;
    lw += dpp<ROR4>(lw); lw += dpp<XOR8>(lw);
    lw += __shfl_xor(lw, 16); lw += __shfl_xor(lw, 32);
    if (r == 0) {
        float* rec = wrec + wave * 136;
        *reinterpret_cast<f32x4*>(rec + 8 + 8 * c) = f32x4{acc[0], acc[1], acc[2], acc[3]};
        *reinterpret_cast<f32x4*>(rec + 12 + 8 * c) = f32x4{acc[4], acc[5], acc[6], acc[7]};
        if (c == 0) { rec[0] = Mw; rec[1] = lw; }
    }
    __syncthreads();

    const bool straddle = (j == 3);
    if (tid < 256) {
        const int seg = tid >> 7, d = tid & 127;
        const int w0 = (straddle && seg == 1) ? 4 : 0, w1 = straddle ? w0 + 4 : 8;
        float o = 0.f, M = -INFINITY, L = 0.f;
        if (seg == 0 || straddle) {
            for (int w = w0; w < w1; ++w) M = fmaxf(M, wrec[w * 136]);
            for (int w = w0; w < w1; ++w) {
                const float sc = exp2a(wrec[w * 136] - M);
                o = fmaf(sc, wrec[w * 136 + 8 + d], o);
                L = fmaf(sc, wrec[w * 136 + 1], L);
            }
            if (d == 0) {
                const int bs = straddle ? seg : (j > 3 ? 1 : 0);
                const int slot = straddle ? (seg == 0 ? 3 : 0) : (j > 3 ? j - 3 : j);
                float2* st = reinterpret_cast<float2*>(p.statA) + ((bs * 32 + h) * 4 + slot);
                *st = make_float2(M, L);
            }
        }
        float* osf = reinterpret_cast<float*>(os);
        osf[2 * d + seg] = o;
    }
    __syncthreads();
    f2_partial(of, os, ybuf, wave, lane);
    __syncthreads();
    {
        const int bs0 = straddle ? 0 : (j > 3 ? 1 : 0);
        const int slot0 = straddle ? 3 : (j > 3 ? j - 3 : j);
        p.yA[(size_t)((bs0 * 32 + h) * 4 + slot0) * 512 + tid] = f2_sum(ybuf, 0, tid);
        if (straddle) p.yA[(size_t)((1 * 32 + h) * 4 + 0) * 512 + tid] = f2_sum(ybuf, 1, tid);
    }
}

constexpr int B_T = 128;
constexpr int B_CSTR = 1024 + 16;
constexpr int B_PSTR = 256 + 16;
constexpr int B_LDS_TILE = 0;
constexpr int B_LDS_P = B_T * B_CSTR;
constexpr int B_LDS_ST = B_LDS_P + 32 * B_PSTR;
constexpr int B_LDS_END3 = B_LDS_ST + 2048;
constexpr int B_LDS_XS = 0;
constexpr int B_LDS_RED = 4096;
constexpr int B_LDS_QPK = B_LDS_RED + 8192;
constexpr int B_LDS_QA = B_LDS_QPK + 512;
constexpr int B_LDS_OS = B_LDS_QA + 2048;
constexpr int B_LDS_WGT = B_LDS_OS + 1024;
constexpr int B_LDS_YBUF = B_LDS_WGT + 512;
constexpr int B_LDS_END4 = B_LDS_YBUF + 32768;

constexpr int LDS_TOTAL = B_LDS_END3 > A_LDS_END ? B_LDS_END3 : A_LDS_END;

__device__ __forceinline__ void signal_add(u32* cnt) {
    __hip_atomic_fetch_add(cnt, 1u, __ATOMIC_RELAXED, __HIP_MEMORY_SCOPE_AGENT);
}
__device__ __forceinline__ void wait_count(u32* cnt, u32 target) {
    for (u32 spins = 0; spins < (1u << 24); ++spins) {
        if (__hip_atomic_load(cnt, __ATOMIC_RELAXED, __HIP_MEMORY_SCOPE_AGENT) >= target) break;
        __builtin_amdgcn_s_sleep(2);
    }
}

__device__ __forceinline__ void roleB12(const Params& p, const int h, unsigned char* smem) {
    const int tid = threadIdx.x, lane = tid & 63, wave = __builtin_amdgcn_readfirstlane(tid >> 6);
    const int r = lane >> 4, c = lane & 15;
    float2* xs = reinterpret_cast<float2*>(smem + B_LDS_XS);
    float* red = reinterpret_cast<float*>(smem + B_LDS_RED);
    u16* qpk = reinterpret_cast<u16*>(smem + B_LDS_QPK);
    u16* qa = reinterpret_cast<u16*>(smem + B_LDS_QA);

    const u16 x0 = p.B_x[tid], x1 = p.B_x[512 + tid];
    u32x4 wf[16], uf[16];
    {
        const u16* W = p.B_wq + (size_t)(64 * wave + r) * 4096 + h * 128 + 8 * c;
#pragma unroll
        for (int i = 0; i < 16; ++i) wf[i] = ldg16(W + (size_t)(4 * i) * 4096);
        const u16* U = p.B_wuk + (size_t)(64 * wave + r) * 4096 + h * 128 + 8 * c;
#pragma unroll
        for (int i = 0; i < 16; ++i) uf[i] = ldg16(U + (size_t)(4 * i) * 4096);
    }
    xs[tid] = make_float2(bf2f(x0), bf2f(x1));
    __syncthreads();
    f1_partial(wf, xs, red, wave, lane);
    __syncthreads();
    if (tid < 256) qpk[tid] = f2bf(f1_sum(red, tid >> 7, tid & 127));
    __syncthreads();
    const u32x4 q0v = *reinterpret_cast<const u32x4*>(qpk + 8 * c);
    const u32x4 q1v = *reinterpret_cast<const u32x4*>(qpk + 128 + 8 * c);
    float v[16][2];
#pragma unroll
    for (int i = 0; i < 16; ++i) {
        float d0 = 0.f, d1 = 0.f;
#pragma unroll
        for (int k = 0; k < 4; ++k) { d0 = dot2(uf[i][k], q0v[k], d0); d1 = dot2(uf[i][k], q1v[k], d1); }
        v[i][0] = d0; v[i][1] = d1;
    }
#define B_TR(HALF, CTRL, BIT) _Pragma("unroll") for (int i = 0; i < (HALF); ++i) _Pragma("unroll") for (int bb = 0; bb < 2; ++bb) { \
        const float lo_ = v[i][bb], hi_ = v[i + (HALF)][bb]; const float a_ = lo_ + dpp<CTRL>(lo_), b_ = hi_ + dpp<CTRL>(hi_); \
        v[i][bb] = (c & (BIT)) ? b_ : a_; }
    B_TR(8, XOR8, 8) B_TR(4, XOR7, 4) B_TR(2, XOR2, 2) B_TR(1, XOR1, 1)
#undef B_TR
    qa[64 * wave + 4 * c + r] = f2bf(v[0][0]);
    qa[512 + 64 * wave + 4 * c + r] = f2bf(v[0][1]);
    __syncthreads();
    if (tid < 128) {
        const int bb = tid >> 6, ch = tid & 63;
        const u32x4 val = *reinterpret_cast<const u32x4*>(qa + bb * 512 + ch * 8);
        auto rs = __builtin_amdgcn_make_buffer_rsrc((void*)p.qabs, 0, 2 * 32 * 512 * 2, 0x00020000);
        __builtin_amdgcn_raw_buffer_store_b128(val, rs, ((bb * 32 + h) * 512 + ch * 8) * 2, 0, 16);
    }
    asm volatile("s_waitcnt vmcnt(0)" ::: "memory");
    __syncthreads();
    if (tid == 0) signal_add(p.flags + 0);
}

__device__ __forceinline__ void roleB3(const Params& p, const int b, const int s, unsigned char* smem) {
    const int tid = threadIdx.x, lane = tid & 63, wave = __builtin_amdgcn_readfirstlane(tid >> 6);
    const int g = lane >> 4, jj = lane & 15;
    unsigned char* tile = smem + B_LDS_TILE;
    unsigned char* pimg = smem + B_LDS_P;
    float* smax = reinterpret_cast<float*>(smem + B_LDS_ST);
    float* salpha = smax + 128;
    float* sl = salpha + 32;
    const int rt = wave & 1, kt0 = wave >> 1;

    const u16* src = p.B_ckv + ((size_t)b * 4096 + 256 * s) * 512;
    u32x4 st[16];
#pragma unroll
    for (int i = 0; i < 16; ++i) { const int n = tid + 512 * i; st[i] = ldg16(src + (size_t)(n >> 6) * 512 + (n & 63) * 8); }
#pragma unroll
    for (int i = 0; i < 16; ++i) { const int n = tid + 512 * i; *reinterpret_cast<u32x4*>(tile + (n >> 6) * B_CSTR + (n & 63) * 16) = st[i]; }

    if (tid == 0) wait_count(p.flags + 0, 32u);
    __syncthreads();
    u32x4 aq[16];
    {
        auto rs = __builtin_amdgcn_make_buffer_rsrc((void*)p.qabs, 0, 2 * 32 * 512 * 2, 0x00020000);
#pragma unroll
        for (int ks = 0; ks < 16; ++ks)
            aq[ks] = __builtin_amdgcn_raw_buffer_load_b128(rs, ((b * 32 + 16 * rt + jj) * 512 + 32 * ks + 8 * g) * 2, 0, 16);
    }

    float mrun[4], lrun[4];
#pragma unroll
    for (int k = 0; k < 4; ++k) { mrun[k] = -INFINITY; lrun[k] = 0.f; }
    f32x4 acc[2][4];
#pragma unroll
    for (int a = 0; a < 2; ++a)
#pragma unroll
        for (int cc = 0; cc < 4; ++cc) acc[a][cc] = f32x4{0.f, 0.f, 0.f, 0.f};

#pragma unroll
    for (int sub = 0; sub < 2; ++sub) {
        if (sub == 1) {
#pragma unroll
            for (int i = 0; i < 16; ++i) { const int n = tid + 512 * i; st[i] = ldg16(src + (size_t)(128 + (n >> 6)) * 512 + (n & 63) * 8); }
            __syncthreads();
#pragma unroll
            for (int i = 0; i < 16; ++i) { const int n = tid + 512 * i; *reinterpret_cast<u32x4*>(tile + (n >> 6) * B_CSTR + (n & 63) * 16) = st[i]; }
            __syncthreads();
        }
        f32x4 sa[2];
#pragma unroll
        for (int kk = 0; kk < 2; ++kk) {
            f32x4 sacc = f32x4{0.f, 0.f, 0.f, 0.f};
            const unsigned char* rowp = tile + (16 * (kt0 + 4 * kk) + jj) * B_CSTR + 16 * g;
#pragma unroll
            for (int ks = 0; ks < 16; ++ks) {
                const u32x4 bf = *reinterpret_cast<const u32x4*>(rowp + 64 * ks);
                sacc = __builtin_amdgcn_mfma_f32_16x16x32_bf16(__builtin_bit_cast(bf16x8, aq[ks]), __builtin_bit_cast(bf16x8, bf), sacc, 0, 0, 0);
            }
            sa[kk] = sacc * C2;
        }
        float wm[4];
#pragma unroll
        for (int k = 0; k < 4; ++k) {
            float x = fmaxf(sa[0][k], sa[1][k]);
            x = fmaxf(x, dpp<XOR8>(x)); x = fmaxf(x, dpp<XOR7>(x)); x = fmaxf(x, dpp<XOR2>(x)); x = fmaxf(x, dpp<XOR1>(x));
            wm[k] = x;
        }
        if (jj == 0) *reinterpret_cast<f32x4*>(smax + wave * 16 + 4 * g) = f32x4{wm[0], wm[1], wm[2], wm[3]};
        __syncthreads();
        float alpha[4];
#pragma unroll
        for (int k = 0; k < 4; ++k) {
            float mt = fmaxf(fmaxf(smax[(rt + 0) * 16 + 4 * g + k], smax[(rt + 2) * 16 + 4 * g + k]),
                             fmaxf(smax[(rt + 4) * 16 + 4 * g + k], smax[(rt + 6) * 16 + 4 * g + k]));
            const float mn = fmaxf(mrun[k], mt);
            alpha[k] = exp2a(mrun[k] - mn);
            mrun[k] = mn;
            const float pa = exp2a(sa[0][k] - mn), pb = exp2a(sa[1][k] - mn);
            lrun[k] = fmaf(lrun[k], alpha[k], pa + pb);
            u16* prow = reinterpret_cast<u16*>(pimg + (16 * rt + 4 * g + k) * B_PSTR);
            prow[16 * kt0 + jj] = f2bf(pa);
            prow[16 * (kt0 + 4) + jj] = f2bf(pb);
        }
        if (wave < 2 && jj == 0) *reinterpret_cast<f32x4*>(salpha + 16 * rt + 4 * g) = f32x4{alpha[0], alpha[1], alpha[2], alpha[3]};
        __syncthreads();
#pragma unroll
        for (int a = 0; a < 2; ++a) {
            const f32x4 al = *reinterpret_cast<const f32x4*>(salpha + 16 * a + 4 * g);
#pragma unroll
            for (int cc = 0; cc < 4; ++cc) acc[a][cc] *= al;
        }
#pragma unroll
        for (int ks = 0; ks < 4; ++ks) {
            const u32x4 pa0 = *reinterpret_cast<const u32x4*>(pimg + jj * B_PSTR + (32 * ks + 8 * g) * 2);
            const u32x4 pa1 = *reinterpret_cast<const u32x4*>(pimg + (16 + jj) * B_PSTR + (32 * ks + 8 * g) * 2);
#pragma unroll
            for (int cc = 0; cc < 4; ++cc) {
                const unsigned char* tp = tile + (32 * ks + 8 * g + (jj >> 2)) * B_CSTR + (64 * wave + 16 * cc + 4 * (jj & 3)) * 2;
                const s16x4 t0 = __builtin_amdgcn_ds_read_tr16_b64_v4i16((lds_s16x4*)(tp));
                const s16x4 t1 = __builtin_amdgcn_ds_read_tr16_b64_v4i16((lds_s16x4*)(tp + 4 * B_CSTR));
                const u32x2 u0 = __builtin_bit_cast(u32x2, t0), u1 = __builtin_bit_cast(u32x2, t1);
                const u32x4 bfr = u32x4{u0[0], u0[1], u1[0], u1[1]};
                acc[0][cc] = __builtin_amdgcn_mfma_f32_16x16x32_bf16(__builtin_bit_cast(bf16x8, pa0), __builtin_bit_cast(bf16x8, bfr), acc[0][cc], 0, 0, 0);
                acc[1][cc] = __builtin_amdgcn_mfma_f32_16x16x32_bf16(__builtin_bit_cast(bf16x8, pa1), __builtin_bit_cast(bf16x8, bfr), acc[1][cc], 0, 0, 0);
            }
        }
    }
#pragma unroll
    for (int k = 0; k < 4; ++k) {
        float x = lrun[k];
        x += dpp<XOR8>(x); x += dpp<XOR7>(x); x += dpp<XOR2>(x); x += dpp<XOR1>(x);
        lrun[k] = x;
    }
    if (jj == 0) *reinterpret_cast<f32x4*>(sl + wave * 16 + 4 * g) = f32x4{lrun[0], lrun[1], lrun[2], lrun[3]};
    __syncthreads();
    float* otile = reinterpret_cast<float*>(tile);
#pragma unroll
    for (int a = 0; a < 2; ++a)
#pragma unroll
        for (int cc = 0; cc < 4; ++cc)
#pragma unroll
            for (int k = 0; k < 4; ++k) otile[(16 * a + 4 * g + k) * 512 + 64 * wave + 16 * cc + jj] = acc[a][cc][k];
    if (wave < 2 && jj == 0) {
#pragma unroll
        for (int k = 0; k < 4; ++k) {
            const int hh = 16 * rt + 4 * g + k;
            const float L = sl[(rt + 0) * 16 + 4 * g + k] + sl[(rt + 2) * 16 + 4 * g + k] + sl[(rt + 4) * 16 + 4 * g + k] + sl[(rt + 6) * 16 + 4 * g + k];
            unsigned long long pk = (unsigned long long)__float_as_uint(mrun[k]) | ((unsigned long long)__float_as_uint(L) << 32);
            __hip_atomic_store(reinterpret_cast<unsigned long long*>(p.statB) + ((b * 16 + s) * 32 + hh), pk, __ATOMIC_RELAXED, __HIP_MEMORY_SCOPE_AGENT);
        }
    }
    __syncthreads();
    {
        auto rd = __builtin_amdgcn_make_buffer_rsrc((void*)(p.partB + (size_t)(b * 16 + s) * 32 * 512), 0, 32 * 512 * 4, 0x00020000);
#pragma unroll
        for (int i = 0; i < 8; ++i) {
            const int n = tid + 512 * i;
            __builtin_amdgcn_raw_buffer_store_b128(*reinterpret_cast<const u32x4*>(otile + n * 4), rd, n * 16, 0, 16);
        }
    }
    asm volatile("s_waitcnt vmcnt(0)" ::: "memory");
    __syncthreads();
    if (tid == 0) signal_add(p.flags + 8);
}

__device__ __forceinline__ void roleB4(const Params& p, const int h, unsigned char* smem) {
    const int tid = threadIdx.x, lane = tid & 63, wave = __builtin_amdgcn_readfirstlane(tid >> 6);
    const int r = lane >> 4, c = lane & 15;
    float2* xs = reinterpret_cast<float2*>(smem + B_LDS_XS);
    float* red = reinterpret_cast<float*>(smem + B_LDS_RED);
    float2* os = reinterpret_cast<float2*>(smem + B_LDS_OS);
    float* wgt = reinterpret_cast<float*>(smem + B_LDS_WGT);
    float2* sst = reinterpret_cast<float2*>(wgt + 32);
    float* ybuf = reinterpret_cast<float*>(smem + B_LDS_YBUF);

    u32x4 vf[16], of[16];
    {
        const u16* U = p.B_wuv + (size_t)(64 * wave + r) * 4096 + h * 128 + 8 * c;
#pragma unroll
        for (int i = 0; i < 16; ++i) vf[i] = ldg16(U + (size_t)(4 * i) * 4096);
        const u16* W = p.B_wo + (size_t)(h * 128 + 16 * wave) * 512 + 8 * lane;
#pragma unroll
        for (int i = 0; i < 16; ++i) of[i] = ldg16(W + (size_t)i * 512);
    }
    if (tid == 0) wait_count(p.flags + 8, 32u);
    __syncthreads();
    float pv[2][16];
#pragma unroll
    for (int b = 0; b < 2; ++b)
#pragma unroll
        for (int s = 0; s < 16; ++s)
            pv[b][s] = __uint_as_float(__hip_atomic_load(reinterpret_cast<u32*>(p.partB) + ((size_t)((b * 16 + s) * 32 + h) * 512 + tid), __ATOMIC_RELAXED, __HIP_MEMORY_SCOPE_AGENT));
    if (tid < 32) {
        const unsigned long long pk = __hip_atomic_load(reinterpret_cast<unsigned long long*>(p.statB) + (tid * 32 + h), __ATOMIC_RELAXED, __HIP_MEMORY_SCOPE_AGENT);
        sst[tid] = make_float2(__uint_as_float((u32)pk), __uint_as_float((u32)(pk >> 32)));
    }
    __syncthreads();
    if (tid < 32) {
        const int b = tid >> 4;
        float M = -INFINITY, L = 0.f;
        for (int s = 0; s < 16; ++s) M = fmaxf(M, sst[b * 16 + s].x);
        for (int s = 0; s < 16; ++s) L = fmaf(exp2a(sst[b * 16 + s].x - M), sst[b * 16 + s].y, L);
        wgt[tid] = exp2a(sst[tid].x - M) / L;
    }
    __syncthreads();
    float c0 = 0.f, c1 = 0.f;
#pragma unroll
    for (int s = 0; s < 16; ++s) { c0 = fmaf(wgt[s], pv[0][s], c0); c1 = fmaf(wgt[16 + s], pv[1][s], c1); }
    xs[tid] = make_float2(c0, c1);
    __syncthreads();
    f1_partial(vf, xs, red, wave, lane);
    __syncthreads();
    if (tid < 256) reinterpret_cast<float*>(os)[2 * (tid & 127) + (tid >> 7)] = f1_sum(red, tid >> 7, tid & 127);
    __syncthreads();
    f2_partial(of, os, ybuf, wave, lane);
    __syncthreads();
    p.yB[(size_t)(0 * 32 + h) * 512 + tid] = f2_sum(ybuf, 0, tid);
    p.yB[(size_t)(1 * 32 + h) * 512 + tid] = f2_sum(ybuf, 1, tid);
}

__global__ void __launch_bounds__(512, 2) k_main(Params p) {
    __shared__ __attribute__((aligned(16))) unsigned char smem[LDS_TOTAL];
    const int bid = blockIdx.x, xcd = bid & 7, loc = bid >> 3;
    if (loc < 4) {
        const int h = xcd * 4 + loc;
        roleB12(p, h, smem);
        __syncthreads();
        roleB3(p, h & 1, h >> 1, smem);
        __syncthreads();
        roleB4(p, h, smem);
    } else {
        const int a = loc - 4;
        roleA(p, xcd * 4 + a / 7, a % 7, smem);
    }
}

__global__ void __launch_bounds__(256) k_final(Params p, u16* out) {
    __shared__ float wsl[128];
    __shared__ float red[4][64];
    const int b = blockIdx.x >> 4, part = (blockIdx.x >> 3) & 1, sl = blockIdx.x & 7;
    const int tid = threadIdx.x, grp = tid >> 6, col = sl * 64 + (tid & 63);
    float acc = 0.f;
    if (part == 0) {
        float yv[32];
        const float* y = p.yA + (size_t)(b * 128 + grp * 32) * 512 + col;
#pragma unroll
        for (int i = 0; i < 32; ++i) yv[i] = y[(size_t)i * 512];
        if (tid < 128) {
            const float2* st = reinterpret_cast<const float2*>(p.statA) + (b * 32 + (tid >> 2)) * 4;
            float M = -INFINITY, L = 0.f;
#pragma unroll
            for (int k = 0; k < 4; ++k) M = fmaxf(M, st[k].x);
#pragma unroll
            for (int k = 0; k < 4; ++k) L = fmaf(exp2a(st[k].x - M), st[k].y, L);
            wsl[tid] = exp2a(st[tid & 3].x - M) / L;
        }
        __syncthreads();
#pragma unroll
        for (int i = 0; i < 32; ++i) acc = fmaf(wsl[grp * 32 + i], yv[i], acc);
    } else {
        const float* y = p.yB + (size_t)(b * 32 + grp * 8) * 512 + col;
#pragma unroll
        for (int i = 0; i < 8; ++i) acc += y[(size_t)i * 512];
    }
    red[grp][tid & 63] = acc;
    __syncthreads();
    if (tid < 64) out[b * 1024 + part * 512 + col] = f2bf(red[0][tid] + red[1][tid] + red[2][tid] + red[3][tid]);
}

extern "C" void kernel_launch(void* const* d_in, const int* in_sizes, int n_in,
                              void* d_out, int out_size, void* d_ws, size_t ws_size,
                              hipStream_t stream) {
    Params p;
    p.A_x = (const u16*)d_in[0]; p.A_k = (const u16*)d_in[1]; p.A_v = (const u16*)d_in[2];
    p.A_wq = (const u16*)d_in[3]; p.A_wo = (const u16*)d_in[4];
    p.B_x = (const u16*)d_in[5]; p.B_ckv = (const u16*)d_in[6]; p.B_wq = (const u16*)d_in[7];
    p.B_wuk = (const u16*)d_in[8]; p.B_wuv = (const u16*)d_in[9]; p.B_wo = (const u16*)d_in[10];
    unsigned char* ws = (unsigned char*)d_ws;
    p.flags = (u32*)ws;
    p.qabs = (u16*)(ws + 4096);
    p.statA = (float*)(ws + 4096 + 65536);
    p.statB = (float*)(ws + 4096 + 65536 + 4096);
    p.yB = (float*)(ws + 131072);
    p.yA = (float*)(ws + 262144);
    p.partB = (float*)(ws + 1048576);
    (void)hipMemsetAsync(p.flags, 0, 64, stream);
    k_main<<<256, 512, 0, stream>>>(p);
    k_final<<<32, 256, 0, stream>>>(p, (u16*)d_out);
}
```

```cpp
#include <hip/hip_runtime.h>
#include <stdint.h>

typedef unsigned short u16;
typedef unsigned int u32;
using u32x4 = __attribute__((ext_vector_type(4))) unsigned;
using u32x2 = __attribute__((ext_vector_type(2))) unsigned;
using f32x4 = __attribute__((ext_vector_type(4))) float;
using bf16x2 = __attribute__((ext_vector_type(2))) __bf16;
using bf16x8 = __attribute__((ext_vector_type(8))) __bf16;
using s16x4 = __attribute__((ext_vector_type(4))) short;
typedef __attribute__((address_space(3))) s16x4 lds_s16x4;

constexpr float SCALE = 0.08838834764831845f;
constexpr float C2 = SCALE * 1.4426950408889634f;

__device__ __forceinline__ float bflo(u32 v) { return __uint_as_float(v << 16); }
__device__ __forceinline__ float bfhi(u32 v) { return __uint_as_float(v & 0xffff0000u); }
__device__ __forceinline__ float bf2f(u16 v) { return __uint_as_float(((u32)v) << 16); }
__device__ __forceinline__ u16 f2bf(float f) {
    u32 u = __float_as_uint(f);
    u += 0x7fffu + ((u >> 16) & 1u);
    return (u16)(u >> 16);
}
__device__ __forceinline__ float dot2(u32 a, u32 b, float acc) {
    return __builtin_amdgcn_fdot2_f32_bf16(__builtin_bit_cast(bf16x2, a), __builtin_bit_cast(bf16x2, b), acc, false);
}
template <int CTRL> __device__ __forceinline__ float dpp(float x) {
    return __builtin_bit_cast(float, __builtin_amdgcn_mov_dpp(__builtin_bit_cast(int, x), CTRL, 0xf, 0xf, true));
}
constexpr int XOR1 = 0xB1, XOR2 = 0x4E, XOR7 = 0x141, XOR8 = 0x128, ROR4 = 0x124;
__device__ __forceinline__ u32x4 ldg16(const u16* p) { return *reinterpret_cast<const u32x4*>(p); }
__device__ __forceinline__ u32x4 ldg16nt(const u16* p) { return __builtin_nontemporal_load(reinterpret_cast<const u32x4*>(p)); }
__device__ __forceinline__ float exp2a(float x) { return __builtin_amdgcn_exp2f(x); }
#define SCHED_FENCE() __builtin_amdgcn_sched_barrier(0)

#define STAMP(idx) do { } while (0)
#define WSTAMP(base) do { } while (0)

struct Params {
    const u16 *A_x, *A_k, *A_v, *A_wq, *A_wo;
    const u16 *B_x, *B_ckv, *B_wq, *B_wuk, *B_wuv, *B_wo;
    u32* flags;
    u16* qabs;
    float* partB;
    float* statB;
    float* yB;
    float* yA;
    float* statA;
};

#define LDS_BARRIER() do { asm volatile("s_waitcnt lgkmcnt(0)" ::: "memory"); __builtin_amdgcn_s_barrier(); asm volatile("" ::: "memory"); } while (0)
#define VM_DRAIN() asm volatile("s_waitcnt vmcnt(0)" ::: "memory")

template <int NB>
__device__ __forceinline__ void f1_partial(const u32x4 (&f)[16], const float* xs, float* red, int wave, int lane) {
    const int r = lane >> 4, c = lane & 15;
    float a[NB][8];
#pragma unroll
    for (int n = 0; n < NB; ++n)
#pragma unroll
        for (int k = 0; k < 8; ++k) a[n][k] = 0.f;
#pragma unroll
    for (int i = 0; i < 16; ++i) {
        float xv[NB];
#pragma unroll
        for (int n = 0; n < NB; ++n) xv[n] = xs[(64 * wave + 4 * i + r) * NB + n];
#pragma unroll
        for (int k = 0; k < 4; ++k) {
            const float lo = bflo(f[i][k]), hi = bfhi(f[i][k]);
#pragma unroll
            for (int n = 0; n < NB; ++n) { a[n][2 * k] = fmaf(xv[n], lo, a[n][2 * k]); a[n][2 * k + 1] = fmaf(xv[n], hi, a[n][2 * k + 1]); }
        }
    }
#pragma unroll
    for (int n = 0; n < NB; ++n)
#pragma unroll
        for (int k = 0; k < 8; ++k) { a[n][k] += __shfl_xor(a[n][k], 16); a[n][k] += __shfl_xor(a[n][k], 32); }
    if (r == 0) {
#pragma unroll
        for (int n = 0; n < NB; ++n) {
            float* d = red + (wave * NB + n) * 128 + 8 * c;
            *reinterpret_cast<f32x4*>(d) = f32x4{a[n][0], a[n][1], a[n][2], a[n][3]};
            *reinterpret_cast<f32x4*>(d + 4) = f32x4{a[n][4], a[n][5], a[n][6], a[n][7]};
        }
    }
}
template <int NB>
__device__ __forceinline__ float f1_sum(const float* red, int n, int d) {
    float s = 0.f;
#pragma unroll
    for (int w = 0; w < 8; ++w) s += red[(w * NB + n) * 128 + d];
    return s;
}

__device__ __forceinline__ void f2_partial(const u32x4 (&f)[16], const float* os, float* ybuf, int wave, int lane) {
    float a0[8];
#pragma unroll
    for (int k = 0; k < 8; ++k) a0[k] = 0.f;
#pragma unroll
    for (int i = 0; i < 16; ++i) {
        const float ov = os[16 * wave + i];
#pragma unroll
        for (int k = 0; k < 4; ++k) { a0[2 * k] = fmaf(ov, bflo(f[i][k]), a0[2 * k]); a0[2 * k + 1] = fmaf(ov, bfhi(f[i][k]), a0[2 * k + 1]); }
    }
    float* d0 = ybuf + wave * 512 + 8 * lane;
    *reinterpret_cast<f32x4*>(d0) = f32x4{a0[0], a0[1], a0[2], a0[3]};
    *reinterpret_cast<f32x4*>(d0 + 4) = f32x4{a0[4], a0[5], a0[6], a0[7]};
}
__device__ __forceinline__ float f2_sum(const float* ybuf, int n) {
    float s = 0.f;
#pragma unroll
    for (int w = 0; w < 8; ++w) s += ybuf[w * 512 + n];
    return s;
}

struct Page { u32x4 k[4], v[4]; };
#define A_DEPTH 2
#define A_HEAD 0

constexpr int A_LDS_XS = 0;
constexpr int A_LDS_RED = 2048;
constexpr int A_LDS_QB = A_LDS_RED + 4096;
constexpr int A_LDS_WREC = A_LDS_QB + 256;
constexpr int A_LDS_OS = A_LDS_WREC + 4352;
constexpr int A_LDS_YBUF = A_LDS_OS + 512;
constexpr int A_LDS_END = A_LDS_YBUF + 16384;

__device__ __forceinline__ void roleA(const Params& p, const int h, const int j, unsigned char* smem) {
    const int tid = threadIdx.x, lane = tid & 63, wave = __builtin_amdgcn_readfirstlane(tid >> 6);
    const int r = lane >> 4, c = lane & 15;
    const int b = j >= 3 ? 1 : 0, t3 = j - 3 * b;
    const int gw = 8 * t3 + wave;
    const int pg0 = (32 * gw) / 3, pg1 = (32 * (gw + 1)) / 3;
    const bool has11 = (pg1 - pg0) == 11;
    const size_t kvoff = ((size_t)(b * 32 + h) * 4096 + 16 * pg0) * 128 + r * 128 + 8 * c;
    const u16* Kp = p.A_k + kvoff;
    const u16* Vp = p.A_v + kvoff;

    float* xs = reinterpret_cast<float*>(smem + A_LDS_XS);
    float* red = reinterpret_cast<float*>(smem + A_LDS_RED);
    u16* qb = reinterpret_cast<u16*>(smem + A_LDS_QB);
    float* wrec = reinterpret_cast<float*>(smem + A_LDS_WREC);
    float* os = reinterpret_cast<float*>(smem + A_LDS_OS);
    float* ybuf = reinterpret_cast<float*>(smem + A_LDS_YBUF);

    STAMP(0);
    const u16 x0 = p.A_x[b * 512 + tid];
    SCHED_FENCE();
    u32x4 wf[16];
    {
        const u16* W = p.A_wq + (size_t)(64 * wave + r) * 4096 + h * 128 + 8 * c;
#pragma unroll
        for (int i = 0; i < 16; ++i) wf[i] = ldg16(W + (size_t)(4 * i) * 4096);
    }
    SCHED_FENCE();
    asm volatile("s_waitcnt vmcnt(%0)" :: "n"(A_HEAD) : "memory");
    SCHED_FENCE();
    STAMP(5);
    Page pg[A_DEPTH];
#define A_ISSUE(S, PAGE) do { _Pragma("unroll") for (int t_ = 0; t_ < 4; ++t_) { \
        pg[S].k[t_] = ldg16nt(Kp + (size_t)((PAGE) * 16 + 4 * t_) * 128); \
        pg[S].v[t_] = ldg16nt(Vp + (size_t)((PAGE) * 16 + 4 * t_) * 128); } } while (0)

    xs[tid] = bf2f(x0);
    LDS_BARRIER();
    SCHED_FENCE();
#pragma unroll
    for (int d_ = 0; d_ < A_DEPTH; ++d_) A_ISSUE(d_, d_);
    SCHED_FENCE();
    STAMP(6);
    f1_partial<1>(wf, xs, red, wave, lane);
    LDS_BARRIER();
    STAMP(7);
    if (tid < 128) qb[tid] = f2bf(f1_sum<1>(red, 0, tid));
    LDS_BARRIER();
    const u32x4 qv = *reinterpret_cast<const u32x4*>(qb + 8 * c);
    SCHED_FENCE();
    STAMP(1);

    u32x4 of[16];
    const u16* Wo = p.A_wo + (size_t)(h * 128 + 16 * wave) * 512 + 8 * lane;

    float m = -INFINITY, l = 0.f, acc[8];
#pragma unroll
    for (int k = 0; k < 8; ++k) acc[k] = 0.f;

#pragma unroll
    for (int i = 0; i < 11; ++i) {
        if (i < 10 || has11) {
            const Page& P = pg[i % A_DEPTH];
            float x[4];
#pragma unroll
            for (int t = 0; t < 4; ++t) {
                float d = dot2(P.k[t][0], qv[0], 0.f);
                d = dot2(P.k[t][1], qv[1], d); d = dot2(P.k[t][2], qv[2], d); d = dot2(P.k[t][3], qv[3], d);
                x[t] = d;
            }
            const float t1 = x[0] + dpp<XOR8>(x[0]), t2 = x[2] + dpp<XOR8>(x[2]);
            const float y0 = (c & 8) ? t2 : t1;
            const float t3a = x[1] + dpp<XOR8>(x[1]), t4 = x[3] + dpp<XOR8>(x[3]);
            const float y1 = (c & 8) ? t4 : t3a;
            const float t5 = y0 + dpp<XOR7>(y0), t6 = y1 + dpp<XOR7>(y1);
            float z = (c & 4) ? t6 : t5;
            z += dpp<XOR2>(z); z += dpp<XOR1>(z);
            const float s = z * C2;
            float pm = fmaxf(s, dpp<ROR4>(s)); pm = fmaxf(pm, dpp<XOR8>(pm));
            const float mn = fmaxf(m, pm);
            const float alpha = exp2a(m - mn), pp = exp2a(s - mn);
            l = fmaf(l, alpha, pp); m = mn;
            const float p0 = dpp<0x150>(pp), p1 = dpp<0x154>(pp), p2 = dpp<0x158>(pp), p3 = dpp<0x15C>(pp);
#pragma unroll
            for (int k = 0; k < 4; ++k) {
                float a0 = acc[2 * k] * alpha, a1 = acc[2 * k + 1] * alpha;
                a0 = fmaf(p0, bflo(P.v[0][k]), a0); a1 = fmaf(p0, bfhi(P.v[0][k]), a1);
                a0 = fmaf(p1, bflo(P.v[1][k]), a0); a1 = fmaf(p1, bfhi(P.v[1][k]), a1);
                a0 = fmaf(p2, bflo(P.v[2][k]), a0); a1 = fmaf(p2, bfhi(P.v[2][k]), a1);
                a0 = fmaf(p3, bflo(P.v[3][k]), a0); a1 = fmaf(p3, bfhi(P.v[3][k]), a1);
                acc[2 * k] = a0; acc[2 * k + 1] = a1;
            }
            asm volatile("" : "+v"(acc[0]), "+v"(acc[1]), "+v"(acc[2]), "+v"(acc[3]), "+v"(acc[4]), "+v"(acc[5]), "+v"(acc[6]), "+v"(acc[7]));
        }
        SCHED_FENCE();
        if (i + A_DEPTH < 10) A_ISSUE(i % A_DEPTH, i + A_DEPTH);
        else if (i + A_DEPTH == 10 && has11) A_ISSUE(i % A_DEPTH, i + A_DEPTH);
        if (i == 7 || i == 8) {
#pragma unroll
            for (int ii = 0; ii < 8; ++ii) of[8 * (i - 7) + ii] = ldg16(Wo + (size_t)(8 * (i - 7) + ii) * 512);
        }
        SCHED_FENCE();
    }
#undef A_ISSUE

    WSTAMP(8);
    float Mw = fmaxf(m, __shfl_xor(m, 16)); Mw = fmaxf(Mw, __shfl_xor(Mw, 32));
    const float fsc = exp2a(m - Mw);
#pragma unroll
    for (int k = 0; k < 8; ++k) {
        float a = acc[k] * fsc;
        a += __shfl_xor(a, 16); a += __shfl_xor(a, 32);
        acc[k] = a;
    }
    float lw = l * fsc;
    lw += dpp<ROR4>(lw); lw += dpp<XOR8>(lw);
    lw += __shfl_xor(lw, 16); lw += __shfl_xor(lw, 32);
    if (r == 0) {
        float* rec = wrec + wave * 136;
        *reinterpret_cast<f32x4*>(rec + 8 + 8 * c) = f32x4{acc[0], acc[1], acc[2], acc[3]};
        *reinterpret_cast<f32x4*>(rec + 12 + 8 * c) = f32x4{acc[4], acc[5], acc[6], acc[7]};
        if (c == 0) { rec[0] = Mw; rec[1] = lw; }
    }
    LDS_BARRIER();
    STAMP(2);

    if (tid < 128) {
        float o = 0.f, M = -INFINITY, L = 0.f;
#pragma unroll
        for (int w = 0; w < 8; ++w) M = fmaxf(M, wrec[w * 136]);
#pragma unroll
        for (int w = 0; w < 8; ++w) {
            const float sc = exp2a(wrec[w * 136] - M);
            o = fmaf(sc, wrec[w * 136 + 8 + tid], o);
            L = fmaf(sc, wrec[w * 136 + 1], L);
        }
        if (tid == 0) reinterpret_cast<float2*>(p.statA)[(b * 32 + h) * 3 + t3] = make_float2(M, L);
        os[tid] = o;
    }
    LDS_BARRIER();
    f2_partial(of, os, ybuf, wave, lane);
    LDS_BARRIER();
    STAMP(4);
    p.yA[(size_t)((b * 32 + h) * 3 + t3) * 512 + tid] = f2_sum(ybuf, tid);
    STAMP(3);
}

constexpr int B_T = 128;
constexpr int B_CSTR = 1024 + 16;
constexpr int B_PSTR = 256 + 16;
constexpr int B_LDS_TILE = 0;
constexpr int B_LDS_P = B_T * B_CSTR;
constexpr int B_LDS_ST = B_LDS_P + 32 * B_PSTR;
constexpr int B_LDS_END3 = B_LDS_ST + 2048;
constexpr int B_LDS_XS = B_LDS_END3;
constexpr int B_LDS_RED = B_LDS_XS + 4096;
constexpr int B_LDS_QPK = B_LDS_RED + 8192;
constexpr int B_LDS_QA = B_LDS_QPK + 512;
constexpr int B_LDS_END12 = B_LDS_QA + 1024;
constexpr int B4_LDS_XS = 0;
constexpr int B4_LDS_RED = 2048;
constexpr int B4_LDS_OS = B4_LDS_RED + 4096;
constexpr int B4_LDS_WGT = B4_LDS_OS + 512;
constexpr int B4_LDS_YBUF = B4_LDS_WGT + 512;

constexpr int LDS_TOTAL = B_LDS_END12 > A_LDS_END ? B_LDS_END12 : A_LDS_END;
static_assert(LDS_TOTAL <= 160 * 1024, "LDS budget");

__device__ __forceinline__ void signal_add(u32* cnt) {
    __hip_atomic_fetch_add(cnt, 1u, __ATOMIC_RELAXED, __HIP_MEMORY_SCOPE_AGENT);
}
__device__ __forceinline__ void wait_count(u32* cnt, u32 target) {
    for (u32 spins = 0; spins < (1u << 24); ++spins) {
        if (__hip_atomic_load(cnt, __ATOMIC_RELAXED, __HIP_MEMORY_SCOPE_AGENT) >= target) break;
        __builtin_amdgcn_s_sleep(2);
    }
}

__device__ __forceinline__ void roleB12(const Params& p, const int h, const int hf, const int tb, const int ts, unsigned char* smem) {
    const int tid = threadIdx.x, lane = tid & 63, wave = __builtin_amdgcn_readfirstlane(tid >> 6);
    const int r = lane >> 4, c = lane & 15;
    float* xs = reinterpret_cast<float*>(smem + B_LDS_XS);
    float* red = reinterpret_cast<float*>(smem + B_LDS_RED);
    u16* qpk = reinterpret_cast<u16*>(smem + B_LDS_QPK);
    u16* qa = reinterpret_cast<u16*>(smem + B_LDS_QA);

    const u16 x0 = p.B_x[tid], x1 = p.B_x[512 + tid];
    u32x4 wf[16], uf[8];
    {
        const u16* W = p.B_wq + (size_t)(64 * wave + r) * 4096 + h * 128 + 8 * c;
#pragma unroll
        for (int i = 0; i < 16; ++i) wf[i] = ldg16(W + (size_t)(4 * i) * 4096);
        const u16* U = p.B_wuk + (size_t)(256 * hf + 32 * wave + r) * 4096 + h * 128 + 8 * c;
#pragma unroll
        for (int i = 0; i < 8; ++i) uf[i] = ldg16(U + (size_t)(4 * i) * 4096);
    }
    xs[2 * tid] = bf2f(x0); xs[2 * tid + 1] = bf2f(x1);
    LDS_BARRIER();
    f1_partial<2>(wf, xs, red, wave, lane);
    LDS_BARRIER();
    if (tid < 256) qpk[tid] = f2bf(f1_sum<2>(red, tid >> 7, tid & 127));
    LDS_BARRIER();
    const u32x4 q0v = *reinterpret_cast<const u32x4*>(qpk + 8 * c);
    const u32x4 q1v = *reinterpret_cast<const u32x4*>(qpk + 128 + 8 * c);
    float v[8][2];
#pragma unroll
    for (int i = 0; i < 8; ++i) {
        float d0 = 0.f, d1 = 0.f;
#pragma unroll
        for (int k = 0; k < 4; ++k) { d0 = dot2(uf[i][k], q0v[k], d0); d1 = dot2(uf[i][k], q1v[k], d1); }
        v[i][0] = d0 + dpp<XOR8>(d0); v[i][1] = d1 + dpp<XOR8>(d1);
    }
#define B_TR(HALF, CTRL, BIT) _Pragma("unroll") for (int i = 0; i < (HALF); ++i) _Pragma("unroll") for (int bb = 0; bb < 2; ++bb) { \
        const float lo_ = v[i][bb], hi_ = v[i + (HALF)][bb]; const float a_ = lo_ + dpp<CTRL>(lo_), b_ = hi_ + dpp<CTRL>(hi_); \
        v[i][bb] = (c & (BIT)) ? b_ : a_; }
    B_TR(4, XOR7, 4) B_TR(2, XOR2, 2) B_TR(1, XOR1, 1)
#undef B_TR
    if (c < 8) {
        qa[32 * wave + 4 * c + r] = f2bf(v[0][0]);
        qa[256 + 32 * wave + 4 * c + r] = f2bf(v[0][1]);
    }
    LDS_BARRIER();
    if (tid < 64) {
        const int bb = tid >> 5, ch = tid & 31;
        const u32x4 val = *reinterpret_cast<const u32x4*>(qa + bb * 256 + ch * 8);
        auto rs = __builtin_amdgcn_make_buffer_rsrc((void*)p.qabs, 0, 2 * 32 * 512 * 2, 0x00020000);
        __builtin_amdgcn_raw_buffer_store_b128(val, rs, ((bb * 32 + h) * 512 + 256 * hf + ch * 8) * 2, 0, 16);
    }
    VM_DRAIN();
    LDS_BARRIER();
    if (tid == 0) signal_add(p.flags + 0);
    SCHED_FENCE();
    {
        const u16* src = p.B_ckv + ((size_t)tb * 4096 + 128 * ts + 16 * wave) * 512 + lane * 8;
#pragma unroll
        for (int i = 0; i < 16; ++i)
            __builtin_amdgcn_global_load_lds((const void*)(src + (size_t)i * 512),
                                             (__attribute__((address_space(3))) void*)(smem + B_LDS_TILE + (16 * wave + i) * B_CSTR), 16, 0, 2);
    }
    SCHED_FENCE();
}

__device__ __forceinline__ void roleB3(const Params& p, const int b, const int s, const int h, unsigned char* smem, u32x4 (&vf)[16], u32x4 (&of)[16]) {
    const int tid = threadIdx.x, lane = tid & 63, wave = __builtin_amdgcn_readfirstlane(tid >> 6);
    const int g = lane >> 4, jj = lane & 15;
    unsigned char* tile = smem + B_LDS_TILE;
    unsigned char* pimg = smem + B_LDS_P;
    float* smax = reinterpret_cast<float*>(smem + B_LDS_ST);
    float* sl = smax + 128;
    const int rt = wave & 1, kt0 = wave >> 1;

    STAMP(2);
    if (tid == 0) wait_count(p.flags + 0, 64u);
    STAMP(3);
    VM_DRAIN();
    LDS_BARRIER();
    u32x4 aq[16];
    {
        auto rs = __builtin_amdgcn_make_buffer_rsrc((void*)p.qabs, 0, 2 * 32 * 512 * 2, 0x00020000);
#pragma unroll
        for (int ks = 0; ks < 16; ++ks)
            aq[ks] = __builtin_amdgcn_raw_buffer_load_b128(rs, ((b * 32 + 16 * rt + jj) * 512 + 32 * ks + 8 * g) * 2, 0, 16);
    }
#pragma unroll
    for (int ks = 0; ks < 16; ks += 4) asm volatile("" : "+v"(aq[ks]), "+v"(aq[ks + 1]), "+v"(aq[ks + 2]), "+v"(aq[ks + 3]));
    SCHED_FENCE();
    f32x4 sa[2];
#pragma unroll
    for (int kk = 0; kk < 2; ++kk) {
        f32x4 sacc = f32x4{0.f, 0.f, 0.f, 0.f};
        const unsigned char* rowp = tile + (16 * (kt0 + 4 * kk) + jj) * B_CSTR + 16 * g;
#pragma unroll
        for (int ks = 0; ks < 16; ++ks) {
            const u32x4 bf = *reinterpret_cast<const u32x4*>(rowp + 64 * ks);
            sacc = __builtin_amdgcn_mfma_f32_16x16x32_bf16(__builtin_bit_cast(bf16x8, aq[ks]), __builtin_bit_cast(bf16x8, bf), sacc, 0, 0, 0);
        }
        sa[kk] = sacc * C2;
    }
    SCHED_FENCE();
    {
        const u16* U = p.B_wuv + (size_t)(64 * wave + g) * 4096 + h * 128 + 8 * jj;
#pragma unroll
        for (int i = 0; i < 16; ++i) vf[i] = ldg16(U + (size_t)(4 * i) * 4096);
    }
    SCHED_FENCE();
    float wm[4];
#pragma unroll
    for (int k = 0; k < 4; ++k) {
        float x = fmaxf(sa[0][k], sa[1][k]);
        x = fmaxf(x, dpp<XOR8>(x)); x = fmaxf(x, dpp<XOR7>(x)); x = fmaxf(x, dpp<XOR2>(x)); x = fmaxf(x, dpp<XOR1>(x));
        wm[k] = x;
    }
    if (jj == 0) *reinterpret_cast<f32x4*>(smax + wave * 16 + 4 * g) = f32x4{wm[0], wm[1], wm[2], wm[3]};
    LDS_BARRIER();
    float mrun[4], lrun[4];
#pragma unroll
    for (int k = 0; k < 4; ++k) {
        const float mn = fmaxf(fmaxf(smax[(rt + 0) * 16 + 4 * g + k], smax[(rt + 2) * 16 + 4 * g + k]),
                               fmaxf(smax[(rt + 4) * 16 + 4 * g + k], smax[(rt + 6) * 16 + 4 * g + k]));
        mrun[k] = mn;
        const float pa = exp2a(sa[0][k] - mn), pb = exp2a(sa[1][k] - mn);
        lrun[k] = pa + pb;
        u16* prow = reinterpret_cast<u16*>(pimg + (16 * rt + 4 * g + k) * B_PSTR);
        prow[16 * kt0 + jj] = f2bf(pa);
        prow[16 * (kt0 + 4) + jj] = f2bf(pb);
    }
#pragma unroll
    for (int k = 0; k < 4; ++k) {
        float x = lrun[k];
        x += dpp<XOR8>(x); x += dpp<XOR7>(x); x += dpp<XOR2>(x); x += dpp<XOR1>(x);
        lrun[k] = x;
    }
    if (jj == 0) *reinterpret_cast<f32x4*>(sl + wave * 16 + 4 * g) = f32x4{lrun[0], lrun[1], lrun[2], lrun[3]};
    LDS_BARRIER();
    f32x4 acc[2][4];
#pragma unroll
    for (int a = 0; a < 2; ++a)
#pragma unroll
        for (int cc = 0; cc < 4; ++cc) acc[a][cc] = f32x4{0.f, 0.f, 0.f, 0.f};
#pragma unroll
    for (int ks = 0; ks < 4; ++ks) {
        const u32x4 pa0 = *reinterpret_cast<const u32x4*>(pimg + jj * B_PSTR + (32 * ks + 8 * g) * 2);
        const u32x4 pa1 = *reinterpret_cast<const u32x4*>(pimg + (16 + jj) * B_PSTR + (32 * ks + 8 * g) * 2);
#pragma unroll
        for (int cc = 0; cc < 4; ++cc) {
            const unsigned char* tp = tile + (32 * ks + 8 * g + (jj >> 2)) * B_CSTR + (64 * wave + 16 * cc + 4 * (jj & 3)) * 2;
            const s16x4 t0 = __builtin_amdgcn_ds_read_tr16_b64_v4i16((lds_s16x4*)(tp));
            const s16x4 t1 = __builtin_amdgcn_ds_read_tr16_b64_v4i16((lds_s16x4*)(tp + 4 * B_CSTR));
            const u32x2 u0 = __builtin_bit_cast(u32x2, t0), u1 = __builtin_bit_cast(u32x2, t1);
            const u32x4 bfr = u32x4{u0[0], u0[1], u1[0], u1[1]};
            acc[0][cc] = __builtin_amdgcn_mfma_f32_16x16x32_bf16(__builtin_bit_cast(bf16x8, pa0), __builtin_bit_cast(bf16x8, bfr), acc[0][cc], 0, 0, 0);
            acc[1][cc] = __builtin_amdgcn_mfma_f32_16x16x32_bf16(__builtin_bit_cast(bf16x8, pa1), __builtin_bit_cast(bf16x8, bfr), acc[1][cc], 0, 0, 0);
        }
    }
    if (wave < 2 && jj == 0) {
#pragma unroll
        for (int k = 0; k < 4; ++k) {
            const int hh = 16 * rt + 4 * g + k;
            const float L = sl[(rt + 0) * 16 + 4 * g + k] + sl[(rt + 2) * 16 + 4 * g + k] + sl[(rt + 4) * 16 + 4 * g + k] + sl[(rt + 6) * 16 + 4 * g + k];
            unsigned long long pk = (unsigned long long)__float_as_uint(mrun[k]) | ((unsigned long long)__float_as_uint(L) << 32);
            __hip_atomic_store(reinterpret_cast<unsigned long long*>(p.statB) + ((b * 32 + s) * 32 + hh), pk, __ATOMIC_RELAXED, __HIP_MEMORY_SCOPE_AGENT);
        }
    }
    LDS_BARRIER();
    float* otile = reinterpret_cast<float*>(tile);
#pragma unroll
    for (int a = 0; a < 2; ++a)
#pragma unroll
        for (int cc = 0; cc < 4; ++cc)
#pragma unroll
            for (int k = 0; k < 4; ++k) otile[(16 * a + 4 * g + k) * 512 + 64 * wave + 16 * cc + jj] = acc[a][cc][k];
    SCHED_FENCE();
    {
        const u16* W = p.B_wo + (size_t)(h * 128 + 16 * wave) * 512 + 8 * lane;
#pragma unroll
        for (int i = 0; i < 16; ++i) of[i] = ldg16(W + (size_t)i * 512);
    }
    SCHED_FENCE();
    LDS_BARRIER();
    {
        auto rd = __builtin_amdgcn_make_buffer_rsrc((void*)(p.partB + (size_t)(b * 32 + s) * 32 * 512), 0, 32 * 512 * 4, 0x00020000);
#pragma unroll
        for (int i = 0; i < 8; ++i) {
            const int n = tid + 512 * i;
            __builtin_amdgcn_raw_buffer_store_b128(*reinterpret_cast<const u32x4*>(otile + n * 4), rd, n * 16, 0, 16);
        }
    }
    VM_DRAIN();
    LDS_BARRIER();
    if (tid == 0) signal_add(p.flags + 8);
}

__device__ __forceinline__ void roleB4(const Params& p, const int h, const int bb, unsigned char* smem, const u32x4 (&vf)[16], const u32x4 (&of)[16]) {
    const int tid = threadIdx.x, lane = tid & 63, wave = __builtin_amdgcn_readfirstlane(tid >> 6);
    const int r = lane >> 4, c = lane & 15;
    float* xs = reinterpret_cast<float*>(smem + B4_LDS_XS);
    float* red = reinterpret_cast<float*>(smem + B4_LDS_RED);
    float* os = reinterpret_cast<float*>(smem + B4_LDS_OS);
    float* wgt = reinterpret_cast<float*>(smem + B4_LDS_WGT);
    float2* sst = reinterpret_cast<float2*>(wgt + 32);
    float* ybuf = reinterpret_cast<float*>(smem + B4_LDS_YBUF);

    STAMP(5);
    if (tid == 0) wait_count(p.flags + 8, 64u);
    STAMP(6);
    LDS_BARRIER();
    float pv[32];
#pragma unroll
    for (int s = 0; s < 32; ++s)
        pv[s] = __uint_as_float(__hip_atomic_load(reinterpret_cast<u32*>(p.partB) + ((size_t)((bb * 32 + s) * 32 + h) * 512 + tid), __ATOMIC_RELAXED, __HIP_MEMORY_SCOPE_AGENT));
    if (tid < 32) {
        const unsigned long long pk = __hip_atomic_load(reinterpret_cast<unsigned long long*>(p.statB) + ((bb * 32 + tid) * 32 + h), __ATOMIC_RELAXED, __HIP_MEMORY_SCOPE_AGENT);
        sst[tid] = make_float2(__uint_as_float((u32)pk), __uint_as_float((u32)(pk >> 32)));
    }
    LDS_BARRIER();
    if (tid < 32) {
        float M = -INFINITY, L = 0.f;
        for (int s = 0; s < 32; ++s) M = fmaxf(M, sst[s].x);
        for (int s = 0; s < 32; ++s) L = fmaf(exp2a(sst[s].x - M), sst[s].y, L);
        wgt[tid] = exp2a(sst[tid].x - M) / L;
    }
    LDS_BARRIER();
    float c0 = 0.f;
#pragma unroll
    for (int s = 0; s < 32; ++s) c0 = fmaf(wgt[s], pv[s], c0);
    xs[tid] = c0;
    LDS_BARRIER();
    f1_partial<1>(vf, xs, red, wave, lane);
    LDS_BARRIER();
    if (tid < 128) os[tid] = f1_sum<1>(red, 0, tid);
    LDS_BARRIER();
    f2_partial(of, os, ybuf, wave, lane);
    LDS_BARRIER();
    p.yB[(size_t)(bb * 32 + h) * 512 + tid] = f2_sum(ybuf, tid);
}

__global__ void __launch_bounds__(512, 2) k_main(Params p) {
    __shared__ __attribute__((aligned(16))) unsigned char smem[LDS_TOTAL];
    const int bid = blockIdx.x, xcd = bid & 7, loc = bid >> 3;
    if (loc < 8) {
        const int h = xcd * 4 + (loc >> 1), hf = loc & 1;
        STAMP(0);
        roleB12(p, h, hf, hf, h, smem);
        STAMP(1);
        u32x4 vf[16], of[16];
        roleB3(p, hf, h, h, smem, vf, of);
        LDS_BARRIER();
        STAMP(4);
        roleB4(p, h, hf, smem, vf, of);
        STAMP(7);
    } else {
        const int a = loc - 8;
        roleA(p, xcd * 4 + a / 6, a % 6, smem);
    }
}

__global__ void __launch_bounds__(256) k_final(Params p, u16* out) {
    __shared__ float wsl[96];
    __shared__ float red[4][64];
    const int b = blockIdx.x >> 4, part = (blockIdx.x >> 3) & 1, sl = blockIdx.x & 7;
    const int tid = threadIdx.x, grp = tid >> 6, col = sl * 64 + (tid & 63);
    float acc = 0.f;
    if (part == 0) {
        float yv[24];
        const float* y = p.yA + (size_t)(b * 96 + grp * 24) * 512 + col;
#pragma unroll
        for (int i = 0; i < 24; ++i) yv[i] = y[(size_t)i * 512];
        if (tid < 96) {
            const int hh = tid / 3;
            const float2* st = reinterpret_cast<const float2*>(p.statA) + (b * 32 + hh) * 3;
            float M = -INFINITY, L = 0.f;
#pragma unroll
            for (int k = 0; k < 3; ++k) M = fmaxf(M, st[k].x);
#pragma unroll
            for (int k = 0; k < 3; ++k) L = fmaf(exp2a(st[k].x - M), st[k].y, L);
            wsl[tid] = exp2a(st[tid - 3 * hh].x - M) / L;
        }
        __syncthreads();
#pragma unroll
        for (int i = 0; i < 24; ++i) acc = fmaf(wsl[grp * 24 + i], yv[i], acc);
    } else {
        const float* y = p.yB + (size_t)(b * 32 + grp * 8) * 512 + col;
#pragma unroll
        for (int i = 0; i < 8; ++i) acc += y[(size_t)i * 512];
    }
    red[grp][tid & 63] = acc;
    __syncthreads();
    if (tid < 64) out[b * 1024 + part * 512 + col] = f2bf(red[0][tid] + red[1][tid] + red[2][tid] + red[3][tid]);
}

extern "C" void kernel_launch(void* const* d_in, const int* in_sizes, int n_in,
                              void* d_out, int out_size, void* d_ws, size_t ws_size,
                              hipStream_t stream) {
    Params p;
    p.A_x = (const u16*)d_in[0]; p.A_k = (const u16*)d_in[1]; p.A_v = (const u16*)d_in[2];
    p.A_wq = (const u16*)d_in[3]; p.A_wo = (const u16*)d_in[4];
    p.B_x = (const u16*)d_in[5]; p.B_ckv = (const u16*)d_in[6]; p.B_wq = (const u16*)d_in[7];
    p.B_wuk = (const u16*)d_in[8]; p.B_wuv = (const u16*)d_in[9]; p.B_wo = (const u16*)d_in[10];
    unsigned char* ws = (unsigned char*)d_ws;
    p.flags = (u32*)ws;
    p.qabs = (u16*)(ws + 4096);
    p.statA = (float*)(ws + 4096 + 65536);
    p.statB = (float*)(ws + 4096 + 65536 + 4096);
    p.yB = (float*)(ws + 131072);
    p.yA = (float*)(ws + 262144);
    p.partB = (float*)(ws + 1048576);
    (void)hipMemsetAsync(p.flags, 0, 64, stream);
    k_main<<<256, 512, 0, stream>>>(p);
    k_final<<<32, 256, 0, stream>>>(p, (u16*)d_out);
}
```
